```python
import torch

def _softmax(x, axis=-1):
    return torch.softmax(x, dim=axis)
def _gelu(x):
    return torch.nn.functional.gelu(x, approximate="tanh")
def _silu(x):
    return torch.nn.functional.silu(x)
def _sigmoid(x):
    return torch.sigmoid(x)
def _layernorm(x, axis=-1, eps=1e-5):
    m = x.mean(dim=axis, keepdim=True)
    v = x.var(dim=axis, keepdim=True, unbiased=False)
    return (x - m) / torch.sqrt(v + eps)
def _scatter_add(tgt, idx, src, axis=0):
    return tgt.clone().index_add_(axis, idx, src)
def _segment_sum(src, seg, n):
    out = torch.zeros((n,) + src.shape[1:], dtype=src.dtype)
    return out.index_add_(0, seg, src)
def _pad(x, pw):
    flat = tuple(v for pair in reversed(pw) for v in pair)
    return torch.nn.functional.pad(x, flat)

kernel_name = 'concat_heads__compressed_kv_cla__compressed_kv_cla__decode'

def setup_inputs():

    _w = (lambda a: (lambda t: t.to(torch.bfloat16) if t.is_floating_point() else t)(torch.as_tensor(a)))
    def _A_setup():
        B, H, Sq, Sk, D, M = 2, 16, 1, 4096, 256, 448
        Dc = 4 * D
        return {
            "x": _w((lambda *s: torch.randn(*s, dtype=torch.bfloat16) * 3.2)(B, Sq, M)),
            "c_kv": _w((lambda *s: torch.randn(*s, dtype=torch.bfloat16) * 0.05)(B, Sk, Dc)),
            "w_q": _w((lambda *s: torch.randn(*s, dtype=torch.bfloat16) * 3.2)(M, H, D) * (M ** -0.5)),
            "w_uk": _w((lambda *s: torch.randn(*s, dtype=torch.bfloat16) * 3.2)(Dc, H, D) * (Dc ** -0.5)),
            "w_uv": _w((lambda *s: torch.randn(*s, dtype=torch.bfloat16) * 0.05)(Dc, H, D) * (Dc ** -0.5)),
            "w_o": _w((lambda *s: torch.randn(*s, dtype=torch.bfloat16) * 0.05)(H, D, M) * ((H * D) ** -0.5)),
        }
    _A_in = _A_setup()

    def _B_setup():
        B, H, Sq, Sk, D, M = 2, 16, 1, 4096, 256, 448
        Dc = 4 * D
        return {
            "x": _w((lambda *s: torch.randn(*s, dtype=torch.bfloat16) * 3.2)(B, Sq, M)),
            "c_kv": _w((lambda *s: torch.randn(*s, dtype=torch.bfloat16) * 0.05)(B, Sk, Dc)),
            "w_q": _w((lambda *s: torch.randn(*s, dtype=torch.bfloat16) * 3.2)(M, H, D) * (M ** -0.5)),
            "w_uk": _w((lambda *s: torch.randn(*s, dtype=torch.bfloat16) * 3.2)(Dc, H, D) * (Dc ** -0.5)),
            "w_uv": _w((lambda *s: torch.randn(*s, dtype=torch.bfloat16) * 0.05)(Dc, H, D) * (Dc ** -0.5)),
            "w_o": _w((lambda *s: torch.randn(*s, dtype=torch.bfloat16) * 0.05)(H, D, M) * ((H * D) ** -0.5)),
        }
    _B_in = _B_setup()

    return {
        "A_x": _A_in["x"],
        "A_c_kv": _A_in["c_kv"],
        "A_w_q": _A_in["w_q"],
        "A_w_uk": _A_in["w_uk"],
        "A_w_uv": _A_in["w_uv"],
        "A_w_o": _A_in["w_o"],
        "B_x": _B_in["x"],
        "B_c_kv": _B_in["c_kv"],
        "B_w_q": _B_in["w_q"],
        "B_w_uk": _B_in["w_uk"],
        "B_w_uv": _B_in["w_uv"],
        "B_w_o": _B_in["w_o"],
    }

def reference(A_x, A_c_kv, A_w_q, A_w_uk, A_w_uv, A_w_o, B_x, B_c_kv, B_w_q, B_w_uk, B_w_uv, B_w_o):
    def _A_ref(x, c_kv, w_q, w_uk, w_uv, w_o):
        B, Sq, M = x.shape[0], x.shape[1], x.shape[2]
        Sk = c_kv.shape[1]
        H, D = w_q.shape[1], w_q.shape[2]
        q = torch.einsum("bqm,mhd->bhqd", x, w_q)
        k = torch.einsum("bkc,chd->bhkd", c_kv, w_uk)
        v = torch.einsum("bkc,chd->bhkd", c_kv, w_uv)
        scores = torch.einsum("bhqd,bhkd->bhqk", q, k) * (D ** -0.5)
        off = Sk - Sq
        qi = torch.arange(Sq)[:, None]
        kj = torch.arange(Sk)[None, :]
        cmask = kj <= (qi + off)
        scores = torch.where(cmask[None, None], scores, -1e9)
        p = _softmax(scores, axis=-1)
        ctx = torch.einsum("bhqk,bhkd->bhqd", p, v)
        return torch.einsum("bhqd,hdm->bqm", ctx, w_o)

    def _B_ref(x, c_kv, w_q, w_uk, w_uv, w_o):
        B, Sq, M = x.shape[0], x.shape[1], x.shape[2]
        Sk = c_kv.shape[1]
        H, D = w_q.shape[1], w_q.shape[2]
        q = torch.einsum("bqm,mhd->bhqd", x, w_q)
        k = torch.einsum("bkc,chd->bhkd", c_kv, w_uk)
        v = torch.einsum("bkc,chd->bhkd", c_kv, w_uv)
        scores = torch.einsum("bhqd,bhkd->bhqk", q, k) * (D ** -0.5)
        off = Sk - Sq
        qi = torch.arange(Sq)[:, None]
        kj = torch.arange(Sk)[None, :]
        cmask = kj <= (qi + off)
        scores = torch.where(cmask[None, None], scores, -1e9)
        p = _softmax(scores, axis=-1)
        ctx = torch.einsum("bhqk,bhkd->bhqd", p, v)
        return torch.einsum("bhqd,hdm->bqm", ctx, w_o)

    a = _A_ref(A_x, A_c_kv, A_w_q, A_w_uk, A_w_uv, A_w_o)
    b = _B_ref(B_x, B_c_kv, B_w_q, B_w_uk, B_w_uv, B_w_o)
    return torch.concatenate([a, b], axis=-1)
```

```cpp
#include <hip/hip_runtime.h>
#include <hip/hip_bf16.h>
#include <stdint.h>
typedef unsigned short u16;
__device__ __forceinline__ float bf2f(u16 v){ return __uint_as_float(((unsigned)v)<<16); }
__device__ __forceinline__ u16 f2bf(float f){ unsigned u=__float_as_uint(f); u += 0x7fffu + ((u>>16)&1u); return (u16)(u>>16); }
constexpr int NB=2,H=16,SK=4096,D=256,M=448,DC=1024;
struct P { const u16* x[2]; const u16* ckv[2]; const u16* wq[2]; const u16* wuk[2]; const u16* wuv[2]; const u16* wo[2]; float* ws; u16* out; };
constexpr size_t QP_OFF=0;
constexpr size_t QA_OFF=QP_OFF+8*2*2*4096;
constexpr size_t OP_OFF=QA_OFF+2*2*16*1024;
constexpr size_t ML_OFF=OP_OFF+(size_t)2*2*64*16*1024;
constexpr size_t CP_OFF=ML_OFF+2*2*64*16*2;
constexpr size_t OUTP_OFF=CP_OFF+8*2*2*4096;

__global__ void k1(P p){
  int mc=blockIdx.x,h=blockIdx.y,br=blockIdx.z,d=threadIdx.x;
  float a0=0,a1=0;
  for(int m=mc*56;m<mc*56+56;++m){ float w=bf2f(p.wq[br][(size_t)m*4096+h*256+d]); a0+=w*bf2f(p.x[br][m]); a1+=w*bf2f(p.x[br][M+m]); }
  p.ws[QP_OFF+((mc*2+br)*2+0)*4096+h*256+d]=a0; p.ws[QP_OFF+((mc*2+br)*2+1)*4096+h*256+d]=a1;
}
__global__ void k2(P p){
  int cc=blockIdx.x,h=blockIdx.y,br=blockIdx.z,t=threadIdx.x,lane=t&63,w=t>>6;
  __shared__ float q[2][256];
  for(int b=0;b<2;++b){ float s=0; for(int mc=0;mc<8;++mc) s+=p.ws[QP_OFF+((mc*2+br)*2+b)*4096+h*256+t]; q[b][t]=bf2f(f2bf(s)); }
  __syncthreads();
  for(int i=0;i<16;++i){ int c=cc*64+w*16+i; const u16* r=p.wuk[br]+((size_t)c*16+h)*256+lane*4;
    float a0=0,a1=0; for(int j=0;j<4;++j){ float wv=bf2f(r[j]); a0+=wv*q[0][lane*4+j]; a1+=wv*q[1][lane*4+j]; }
    for(int o=32;o;o>>=1){ a0+=__shfl_xor(a0,o); a1+=__shfl_xor(a1,o);}
    if(lane==0){ p.ws[QA_OFF+((br*2+0)*16+h)*1024+c]=a0*0.0625f; p.ws[QA_OFF+((br*2+1)*16+h)*1024+c]=a1*0.0625f; } }
}
__global__ void __launch_bounds__(256) k3(P p){
  int ch=blockIdx.x,b=blockIdx.y,br=blockIdx.z,t=threadIdx.x,lane=t&63,w=t>>6;
  __shared__ float qa[16][1024]; __shared__ float S[16][64];
  for(int i=t;i<16*1024;i+=256) qa[i>>10][i&1023]=p.ws[QA_OFF+(br*2+b)*16*1024+i];
  __syncthreads();
  const u16* ck=p.ckv[br]+((size_t)b*SK+ch*64)*DC;
  for(int k=w;k<64;k+=4){ float acc[16]; for(int h=0;h<16;++h) acc[h]=0;
    for(int c=lane;c<DC;c+=64){ float v=bf2f(ck[(size_t)k*DC+c]); for(int h=0;h<16;++h) acc[h]+=v*qa[h][c]; }
    for(int h=0;h<16;++h){ float a=acc[h]; for(int o=32;o;o>>=1) a+=__shfl_xor(a,o); if(lane==0) S[h][k]=a; } }
  __syncthreads();
  if(t<16){ float m=-INFINITY; for(int k=0;k<64;++k) m=fmaxf(m,S[t][k]); float l=0; for(int k=0;k<64;++k){ float e=__expf(S[t][k]-m); S[t][k]=e; l+=e; }
    p.ws[ML_OFF+(((br*2+b)*64+ch)*16+t)*2]=m; p.ws[ML_OFF+(((br*2+b)*64+ch)*16+t)*2+1]=l; }
  __syncthreads();
  for(int c=t;c<DC;c+=256){ float acc[16]; for(int h=0;h<16;++h) acc[h]=0;
    for(int k=0;k<64;++k){ float v=bf2f(ck[(size_t)k*DC+c]); for(int h=0;h<16;++h) acc[h]+=v*S[h][k]; }
    for(int h=0;h<16;++h) p.ws[OP_OFF+((((size_t)br*2+b)*64+ch)*16+h)*1024+c]=acc[h]; }
}
__global__ void k4(P p){
  int cr=blockIdx.x,h=blockIdx.y,br=blockIdx.z,t=threadIdx.x;
  __shared__ float cc[2][128]; __shared__ float f[2][64];
  if(t<2){ int b=t; float m=-INFINITY; for(int ch=0;ch<64;++ch) m=fmaxf(m,p.ws[ML_OFF+(((br*2+b)*64+ch)*16+h)*2]); float L=0;
    for(int ch=0;ch<64;++ch){ float e=__expf(p.ws[ML_OFF+(((br*2+b)*64+ch)*16+h)*2]-m); f[b][ch]=e; L+=e*p.ws[ML_OFF+(((br*2+b)*64+ch)*16+h)*2+1]; }
    for(int ch=0;ch<64;++ch) f[b][ch]/=L; }
  __syncthreads();
  { int b=t>>7,c=t&127; float s=0; for(int ch=0;ch<64;++ch) s+=f[b][ch]*p.ws[OP_OFF+((((size_t)br*2+b)*64+ch)*16+h)*1024+cr*128+c]; cc[b][c]=s; }
  __syncthreads();
  float a0=0,a1=0; for(int c=0;c<128;++c){ float wv=bf2f(p.wuv[br][((size_t)(cr*128+c)*16+h)*256+t]); a0+=wv*cc[0][c]; a1+=wv*cc[1][c]; }
  p.ws[CP_OFF+((cr*2+br)*2+0)*4096+h*256+t]=a0; p.ws[CP_OFF+((cr*2+br)*2+1)*4096+h*256+t]=a1;
}
__global__ void k5(P p){
  int j=blockIdx.x,br=blockIdx.y,t=threadIdx.x;
  __shared__ float cx[2][32];
  if(t<64){ int b=t>>5,d=t&31; float s=0; for(int cr=0;cr<8;++cr) s+=p.ws[CP_OFF+((cr*2+br)*2+b)*4096+j*32+d]; cx[b][d]=bf2f(f2bf(s)); }
  __syncthreads();
  float a0=0,a1=0; for(int d=0;d<32;++d){ float wv=bf2f(p.wo[br][(size_t)(j*32+d)*M+t]); a0+=wv*cx[0][d]; a1+=wv*cx[1][d]; }
  p.ws[OUTP_OFF+((br*128+j)*2+0)*M+t]=a0; p.ws[OUTP_OFF+((br*128+j)*2+1)*M+t]=a1;
}
__global__ void k6(P p){
  int b=blockIdx.x,br=blockIdx.y,t=threadIdx.x; float s=0; for(int j=0;j<128;++j) s+=p.ws[OUTP_OFF+((br*128+j)*2+b)*M+t];
  p.out[b*2*M+br*M+t]=f2bf(s);
}
extern "C" void kernel_launch(void* const* d_in, const int* in_sizes, int n_in, void* d_out, int out_size, void* d_ws, size_t ws_size, hipStream_t stream){
  P p; for(int br=0;br<2;++br){ p.x[br]=(const u16*)d_in[br*6+0]; p.ckv[br]=(const u16*)d_in[br*6+1]; p.wq[br]=(const u16*)d_in[br*6+2]; p.wuk[br]=(const u16*)d_in[br*6+3]; p.wuv[br]=(const u16*)d_in[br*6+4]; p.wo[br]=(const u16*)d_in[br*6+5]; }
  p.ws=(float*)d_ws; p.out=(u16*)d_out;
  k1<<<dim3(8,16,2),256,0,stream>>>(p);
  k2<<<dim3(16,16,2),256,0,stream>>>(p);
  k3<<<dim3(64,2,2),256,0,stream>>>(p);
  k4<<<dim3(8,16,2),256,0,stream>>>(p);
  k5<<<dim3(128,2),448,0,stream>>>(p);
  k6<<<dim3(2,2),448,0,stream>>>(p);
}
```

```cpp
#include <hip/hip_runtime.h>
#include <hip/hip_bf16.h>
#include <stdint.h>

namespace mla {
typedef unsigned v4u __attribute__((ext_vector_type(4)));
typedef unsigned v2u __attribute__((ext_vector_type(2)));
typedef float f32x4 __attribute__((ext_vector_type(4)));
typedef float f32x2 __attribute__((ext_vector_type(2)));
typedef short bf16x8 __attribute__((ext_vector_type(8)));
typedef short s16x4 __attribute__((ext_vector_type(4)));
typedef __bf16 bf16x2_t __attribute__((ext_vector_type(2)));
#define LAS __attribute__((address_space(3)))
#define GAS __attribute__((address_space(1)))
typedef __amdgpu_buffer_rsrc_t rsrc_t;

constexpr int NWG = 256, NTHR = 768;
constexpr int AUX_SC1 = 16;
#define AUX_W 0
#define AUX_C 0
constexpr int EP_OFF = 0;
constexpr int QF_OFF = 1024;
constexpr int Q_OFF = 8192;
constexpr int QA_OFF = Q_OFF + 262144;
constexpr int P_OFF = QA_OFF + 131072;
constexpr int PS_OFF = P_OFF + 524288;
constexpr int O_OFF = PS_OFF + 65536;
constexpr int OS_OFF = O_OFF + 1048576;
constexpr int OUT_OFF = OS_OFF + 32768;
constexpr int WS_BYTES = OUT_OFF + 2 * 28 * 16384;
constexpr int STAMP_BYTES = 0;
constexpr int L_QA = 0;
constexpr int L_ST2 = 32768;
constexpr int L_WO = 0;
constexpr int L_RED = 65536;
constexpr int L_ST1 = 131072;
constexpr int L_P1 = 147456;
constexpr int L_CX = L_P1 + 4096;
constexpr int L_FIN = L_CX + 2048;
constexpr int L_ML = L_FIN + 1024;
constexpr int L_Q = L_ML + 1024;
constexpr int L_CNT = L_Q + 1024;
constexpr int LDS_BYTES = L_CNT + 64;
enum { C_P1 = 0, C_QA, C_S, C_ISS, C_SDONE, C_PV, C_WO, C_CX, C_OUT, C_FIN, C_Q, C_FRONT, C_N };
constexpr unsigned SPIN_CAP = 1u << 14;
constexpr unsigned LDS_SPIN_CAP = 1u << 20;
constexpr int OOB = 0x7FF00000;

struct Params { const void* in[12]; void* ws; void* out; };

__device__ __forceinline__ f32x4 mfma(v4u a, v4u b, f32x4 c) {
  return __builtin_amdgcn_mfma_f32_16x16x32_bf16(__builtin_bit_cast(bf16x8, a), __builtin_bit_cast(bf16x8, b), c, 0, 0, 0);
}
__device__ __forceinline__ unsigned pack_bf16(float a, float b) {
  f32x2 v = {a, b};
  return __builtin_bit_cast(unsigned, __builtin_convertvector(v, bf16x2_t));
}
__device__ __forceinline__ float bf_lo(unsigned u) { return __builtin_bit_cast(float, u << 16); }
__device__ __forceinline__ float bf_hi(unsigned u) { return __builtin_bit_cast(float, u & 0xffff0000u); }
__device__ __forceinline__ unsigned fu(float f) { return __builtin_bit_cast(unsigned, f); }
__device__ __forceinline__ float uf(unsigned u) { return __builtin_bit_cast(float, u); }
template <int AUX> __device__ __forceinline__ v4u ld(rsrc_t rs, int voff, int soff) {
  return __builtin_amdgcn_raw_buffer_load_b128(rs, voff, soff, AUX);
}
__device__ __forceinline__ void st_sc1(v4u v, rsrc_t rs, int voff) { __builtin_amdgcn_raw_buffer_store_b128(v, rs, voff, 0, AUX_SC1); }
__device__ __forceinline__ void st_sc1(v2u v, rsrc_t rs, int voff) { __builtin_amdgcn_raw_buffer_store_b64(v, rs, voff, 0, AUX_SC1); }
__device__ __forceinline__ v2u tr_read(LAS unsigned char* p) {
  return __builtin_bit_cast(v2u, __builtin_amdgcn_ds_read_tr16_b64_v4i16((LAS s16x4*)p));
}
__device__ __forceinline__ v4u tr_frag(LAS unsigned char* p, int hi_off) {
  const v2u lo = tr_read(p), hi = tr_read(p + hi_off);
  return v4u{lo.x, lo.y, hi.x, hi.y};
}
template <int CTRL> __device__ __forceinline__ float dpp(float v) {
  return __builtin_bit_cast(float, __builtin_amdgcn_update_dpp(0, __builtin_bit_cast(int, v), CTRL, 0xf, 0xf, true));
}
__device__ __forceinline__ float sum8(float v) { v += dpp<0xB1>(v); v += dpp<0x4E>(v); v += dpp<0x141>(v); return v; }
__device__ __forceinline__ float max8(float v) { v = fmaxf(v, dpp<0xB1>(v)); v = fmaxf(v, dpp<0x4E>(v)); v = fmaxf(v, dpp<0x141>(v)); return v; }
#define PIN() __builtin_amdgcn_sched_barrier(0)
template <int N> __device__ __forceinline__ bool tags_ok(const v4u* u, unsigned tag) {
  unsigned bad = 0;
#pragma unroll
  for (int n = 0; n < N; ++n) bad |= (u[n].y ^ tag) | (u[n].w ^ tag);
  return __all(bad == 0u);
}
__device__ __forceinline__ void arrive(LAS unsigned char* L, int c, int lane) {
  asm volatile("s_waitcnt lgkmcnt(0)" ::: "memory");
  if (lane == 0) __hip_atomic_fetch_add((LAS unsigned*)(L + L_CNT) + c, 1u, __ATOMIC_RELAXED, __HIP_MEMORY_SCOPE_WORKGROUP);
  asm volatile("" ::: "memory");
}
template <int SLEEP> __device__ __forceinline__ void await(LAS unsigned char* L, int c, unsigned n) {
  asm volatile("" ::: "memory");
  for (unsigned spins = 0; spins < LDS_SPIN_CAP; ++spins) {
    const unsigned v = __builtin_amdgcn_readfirstlane(__hip_atomic_load((LAS unsigned*)(L + L_CNT) + c, __ATOMIC_RELAXED, __HIP_MEMORY_SCOPE_WORKGROUP));
    if (v >= n) break;
    if (SLEEP) __builtin_amdgcn_s_sleep(SLEEP);
  }
  asm volatile("" ::: "memory");
}
#define STAMP(k)
#define COUNT(k)
#define RETRY(OK, cnt, GATHER) \
  if (!(OK)) { \
    unsigned spins = 0; \
    do { COUNT(cnt); __builtin_amdgcn_s_sleep(POLL_SLEEP); GATHER } while (!(OK) && ++spins < SPIN_CAP); \
  }
#define POLL_SLEEP 2

__global__ void __launch_bounds__(NTHR, 3) mla_kernel(Params p) {
  extern __shared__ __attribute__((aligned(16))) unsigned char smem[];
  LAS unsigned char* L = (LAS unsigned char*)smem;
  STAMP(0);
  const int tid = threadIdx.x, l = tid & 63, wv = __builtin_amdgcn_readfirstlane(tid >> 6);
  const int r = l & 15, g = l >> 4, q4 = r >> 2, p4 = l & 3;
  const int bid = blockIdx.x, x = bid & 7, s = bid >> 3;
  const int br = x >> 2;
  const int h = 4 * (x & 3) + (s >> 3), j = s & 7, dq = j & 3, hf = j >> 2;
  const int b = (x >> 1) & 1, kg = 4 * (x & 1) + (s >> 3), i = s & 7;
  const int grp = (br * 2 + b) * 8 + kg, prod = grp * 8 + i, col_own = b * 8 + kg;
  if (tid < C_N) ((LAS unsigned*)(L + L_CNT))[tid] = 0u;
  const rsrc_t rS = __builtin_amdgcn_make_buffer_rsrc(p.ws, 0, WS_BYTES + STAMP_BYTES, 0x00020000);
  const unsigned ep = __builtin_amdgcn_raw_buffer_load_b32(rS, EP_OFF + 256 * br, 0, AUX_SC1);
  const int t128_r = (8 * g + q4) * 128 + p4 * 8, t128_f = ((q4 >> 1) & 1) | ((g & 1) << 1);

  if (wv >= 4) {
    const int w = wv - 4;
    __builtin_amdgcn_s_setprio(3);
    const rsrc_t rX = __builtin_amdgcn_make_buffer_rsrc((void*)p.in[br * 6 + 0], 0, 2 * 448 * 2, 0x00020000);
    const rsrc_t rC = __builtin_amdgcn_make_buffer_rsrc((void*)p.in[br * 6 + 1], 0, 2 * 4096 * 1024 * 2, 0x00020000);
    const rsrc_t rWQ = __builtin_amdgcn_make_buffer_rsrc((void*)p.in[br * 6 + 2], 0, 448 * 4096 * 2, 0x00020000);
    const rsrc_t rO = __builtin_amdgcn_make_buffer_rsrc(p.out, 0, 1792 * 2, 0x00020000);
    LAS unsigned char* st = L + L_ST2 + w * 4096;
    {
      const int m0 = 224 * hf + 32 * w;
      const v4u xf = ld<0>(rX, w < 7 ? ((l & 1) * 448 + m0 + 8 * g) * 2 : OOB, 0);
      const int vo = w < 7 ? (m0 + (l >> 3)) * 8192 + (256 * h + 64 * dq) * 2 + (l & 7) * 16 : OOB;
      v4u wq[4];
#pragma unroll
      for (int n = 0; n < 4; ++n) wq[n] = ld<AUX_W>(rWQ, vo, n * 8 * 8192);
      PIN();
      asm volatile("s_waitcnt lgkmcnt(0)" ::: "memory");
      __builtin_amdgcn_s_barrier();
      asm volatile("" ::: "memory");
      STAMP(1);
#pragma unroll
      for (int n = 0; n < 4; ++n) {
        const int row = 8 * n + (l >> 3), ch = l & 7, f = ((row >> 1) & 1) | (((row >> 3) & 1) << 1);
        *(LAS v4u*)(st + row * 128 + (((ch >> 1) ^ f) * 32) + (ch & 1) * 16) = wq[n];
      }
      LAS float* o = (LAS float*)(L + L_P1) + w * 128 + l;
#pragma unroll
      for (int dt = 0; dt < 4; ++dt) {
        const f32x4 a = mfma(xf, tr_frag(st + t128_r + ((dt ^ t128_f) * 32), 512), f32x4{0.f, 0.f, 0.f, 0.f});
        if (l < 16) { o[16 * dt] = a[0]; o[64 + 16 * dt] = a[1]; }
      }
      arrive(L, C_P1, l);
    }
    unsigned tag = (unsigned)__builtin_amdgcn_readfirstlane(ep) + 1u;
    if (tag == 0u) tag = 1u;
    {
      f32x2 a = {0.f, 0.f};
      if (w == 7) {
        await<0>(L, C_P1, 8);
#pragma unroll
        for (int ww = 0; ww < 7; ++ww) a += *(LAS f32x2*)(L + L_P1 + (ww * 128 + 2 * l) * 4);
      }
      st_sc1(v4u{fu(a.x), tag, fu(a.y), tag}, rS, w == 7 ? Q_OFF + ((((br * 16 + h) * 2 + hf) * 2 + (l >> 5)) * 128 + 32 * dq + (l & 31)) * 16 : OOB);
    }
    PIN();
    __builtin_amdgcn_s_setprio(1);
    STAMP(2);
    v4u raw[2][4], v1[4][4], v2[2][8], vt[2][8];
    const int vo1 = ((b * 4096 + 512 * kg + 64 * i + 4 * g) * 1024 + 128 * w + 8 * r) * 2;
    const int vo2 = ((b * 4096 + 512 * kg + 64 * w + 4 * g) * 1024 + 128 * i + 8 * r) * 2;
#define ISSUE_C(kt) \
    _Pragma("unroll") for (int n = 0; n < 4; ++n) { \
      raw[(kt) & 1][n] = ld<AUX_C>(rC, vo1, (16 * (kt) + n) * 2048); \
      v2[(kt) >> 1][4 * ((kt) & 1) + n] = ld<AUX_C>(rC, vo2, (16 * (kt) + n) * 2048); \
    } \
    PIN();
#define RELAY_C(kt) \
    _Pragma("unroll") for (int n = 0; n < 4; ++n) *(LAS v4u*)(st + (4 * g + n) * 256 + ((r ^ (4 * g + n)) * 16)) = raw[(kt) & 1][n]; \
    _Pragma("unroll") for (int ks = 0; ks < 4; ++ks) v1[kt][ks] = *(LAS v4u*)(st + r * 256 + (((4 * ks + g) ^ r) * 16)); \
    asm volatile("" : "+v"(v1[kt][0]), "+v"(v1[kt][1]), "+v"(v1[kt][2]), "+v"(v1[kt][3]) :: "memory"); \
    PIN();
#define TRANSPOSE(ks2) \
    _Pragma("unroll") for (int qq = 0; qq < 4; ++qq) \
      _Pragma("unroll") for (int n = 0; n < 4; ++n) { \
        vt[ks2][2 * qq][n] = __builtin_amdgcn_perm(v2[ks2][2 * n + 1][qq], v2[ks2][2 * n][qq], 0x05040100u); \
        vt[ks2][2 * qq + 1][n] = __builtin_amdgcn_perm(v2[ks2][2 * n + 1][qq], v2[ks2][2 * n][qq], 0x07060302u); \
      } \
    asm volatile("" : "+v"(vt[ks2][0]), "+v"(vt[ks2][1]), "+v"(vt[ks2][2]), "+v"(vt[ks2][3]), "+v"(vt[ks2][4]), "+v"(vt[ks2][5]), "+v"(vt[ks2][6]), "+v"(vt[ks2][7])); \
    PIN();
    await<1>(L, C_FRONT, 4);
    ISSUE_C(0)
    ISSUE_C(1)
    RELAY_C(0)
    ISSUE_C(2)
    RELAY_C(1)
    TRANSPOSE(0)
    ISSUE_C(3)
    if (l == 0) __hip_atomic_fetch_add((LAS unsigned*)(L + L_CNT) + C_ISS, 1u, __ATOMIC_RELAXED, __HIP_MEMORY_SCOPE_WORKGROUP);
    __builtin_amdgcn_s_setprio(3);
    STAMP(3);
    RELAY_C(2)
    await<1>(L, C_QA, 4);
    STAMP(4);
    v4u qf[4];
#pragma unroll
    for (int ks = 0; ks < 4; ++ks) qf[ks] = *(LAS v4u*)(L + L_QA + ((w * 4 + ks) * 64 + l) * 16);
    f32x4 sacc[3];
#pragma unroll
    for (int kt = 0; kt < 3; ++kt) {
      sacc[kt] = f32x4{0.f, 0.f, 0.f, 0.f};
#pragma unroll
      for (int ks = 0; ks < 4; ++ks) sacc[kt] = mfma(v1[kt][ks], qf[ks], sacc[kt]);
    }
    PIN();
    RELAY_C(3)
    {
      f32x4 acc = {0.f, 0.f, 0.f, 0.f};
#pragma unroll
      for (int ks = 0; ks < 4; ++ks) acc = mfma(v1[3][ks], qf[ks], acc);
#pragma unroll
      for (int kt = 0; kt < 3; ++kt) *(LAS f32x4*)(st + (kt * 64 + l) * 16) = sacc[kt];
      *(LAS f32x4*)(st + (3 * 64 + l) * 16) = acc;
    }
    arrive(L, C_S, l);
    STAMP(5);
    {
      v4u pu = {0u, 0u, 0u, 0u};
      v2u ps = {0u, 0u};
      if (w < 2) {
        await<0>(L, C_S, 8);
        f32x4 s0 = {0.f, 0.f, 0.f, 0.f}, s1 = {0.f, 0.f, 0.f, 0.f};
#pragma unroll
        for (int ww = 0; ww < 8; ++ww) {
          s0 += *(LAS f32x4*)(L + L_ST2 + ww * 4096 + ((2 * w) * 64 + l) * 16);
          s1 += *(LAS f32x4*)(L + L_ST2 + ww * 4096 + ((2 * w + 1) * 64 + l) * 16);
        }
        float m = fmaxf(fmaxf(fmaxf(s0[0], s0[1]), fmaxf(s0[2], s0[3])), fmaxf(fmaxf(s1[0], s1[1]), fmaxf(s1[2], s1[3])));
        m = fmaxf(m, __shfl_xor(m, 16));
        m = fmaxf(m, __shfl_xor(m, 32));
        float e[8];
#pragma unroll
        for (int k = 0; k < 4; ++k) { e[k] = __builtin_amdgcn_exp2f(s0[k] - m); e[4 + k] = __builtin_amdgcn_exp2f(s1[k] - m); }
        pu = v4u{pack_bf16(e[0], e[1]) | 0x8000u, pack_bf16(e[2], e[3]), pack_bf16(e[4], e[5]) | 0x8000u, pack_bf16(e[6], e[7])};
        float ls = ((e[0] + e[1]) + (e[2] + e[3])) + ((e[4] + e[5]) + (e[6] + e[7]));
        ls += __shfl_xor(ls, 16);
        ls += __shfl_xor(ls, 32);
        ps = v2u{fu(m), fu(ls) | 1u};
        arrive(L, C_SDONE, l);
      }
      st_sc1(pu, rS, w < 2 ? P_OFF + (prod * 2 + w) * 1024 + l * 16 : OOB);
      st_sc1(ps, rS, (w < 2 && l < 16) ? PS_OFF + prod * 256 + r * 16 + w * 8 : OOB);
    }
    STAMP(6);
    TRANSPOSE(1)
    v4u pg[3];
    const int a_pg = P_OFF + (grp * 8 + w) * 2048 + l * 16;
    const int a_psg = PS_OFF + (grp * 8 + w) * 256 + r * 16;
#define P_GATHER pg[0] = ld<AUX_SC1>(rS, a_pg, 0); pg[1] = ld<AUX_SC1>(rS, a_pg + 1024, 0); pg[2] = ld<AUX_SC1>(rS, a_psg, 0);
#define P_OK __all(((pg[0].x & pg[0].z & pg[1].x & pg[1].z & 0x8000u) != 0u) && ((pg[2].y & pg[2].w & 1u) != 0u))
    P_GATHER
    RETRY(P_OK, 18, P_GATHER)
    STAMP(7);
    {
      const float m0 = uf(pg[2].x), m1 = uf(pg[2].z), mw = fmaxf(m0, m1);
      const float f0 = __builtin_amdgcn_exp2f(m0 - mw), f1 = __builtin_amdgcn_exp2f(m1 - mw);
      const v4u pf0 = {pg[0].x & ~0x8000u, pg[0].y, pg[0].z & ~0x8000u, pg[0].w}, pf1 = {pg[1].x & ~0x8000u, pg[1].y, pg[1].z & ~0x8000u, pg[1].w};
      f32x4 a0[8], a1[8];
#pragma unroll
      for (int ii = 0; ii < 8; ++ii) {
        a0[ii] = mfma(vt[0][ii], pf0, f32x4{0.f, 0.f, 0.f, 0.f});
        a1[ii] = mfma(vt[1][ii], pf1, f32x4{0.f, 0.f, 0.f, 0.f});
      }
      if (l < 16) *(LAS f32x2*)(L + L_ML + (w * 16 + l) * 8) = f32x2{mw, uf(pg[2].y) * f0 + uf(pg[2].w) * f1};
#pragma unroll
      for (int rr = 0; rr < 4; ++rr)
#pragma unroll
        for (int iq = 0; iq < 2; ++iq) {
          f32x4 v;
#pragma unroll
          for (int e = 0; e < 4; ++e) v[e] = a0[4 * iq + e][rr] * f0 + a1[4 * iq + e][rr] * f1;
          const int slot = rr * 2 + iq;
          *(LAS f32x4*)(L + L_RED + ((w * 8 + slot) * 64 + 16 * g + ((r + slot + 8 * (g & 1)) & 15)) * 16) = v;
        }
    }
    arrive(L, C_PV, l);
    await<0>(L, C_PV, 8);
    {
      const int oh = 2 * w + (l >> 5), oQ = l & 31, slot = oQ & 7, gq = oQ >> 3;
      const int a = L_RED + (slot * 64 + 16 * gq + ((oh + slot + 8 * (gq & 1)) & 15)) * 16;
      f32x2 ml[8];
      f32x4 pv[8];
#pragma unroll
      for (int ww = 0; ww < 8; ++ww) ml[ww] = *(LAS f32x2*)(L + L_ML + (ww * 16 + oh) * 8);
#pragma unroll
      for (int ww = 0; ww < 8; ++ww) pv[ww] = *(LAS f32x4*)(L + a + ww * 8192);
      float M = ml[0].x;
#pragma unroll
      for (int ww = 1; ww < 8; ++ww) M = fmaxf(M, ml[ww].x);
      f32x4 v = {0.f, 0.f, 0.f, 0.f};
      float Ls = 0.f;
#pragma unroll
      for (int ww = 0; ww < 8; ++ww) {
        const float f = __builtin_amdgcn_exp2f(ml[ww].x - M);
        v += pv[ww] * f;
        Ls += ml[ww].y * f;
      }
      st_sc1(v2u{pack_bf16(v[0], v[1]) | 1u, pack_bf16(v[2], v[3])}, rS, O_OFF + ((br * 16 + oh) * 16 + col_own) * 2048 + i * 256 + oQ * 8);
      st_sc1(v2u{fu(M), fu(Ls) | 1u}, rS, oQ == 0 ? OS_OFF + (((br * 16 + oh) * 8 + i) * 16 + col_own) * 8 : OOB);
    }
    STAMP(8);
    const int mblk = s * 4 + (x & 3);
    if (mblk < 28) {
      await<2>(L, C_OUT, 4);
      v4u fg[2];
      const int a_f = OUT_OFF + ((br * 28 + mblk) * 128 + 16 * w) * 128 + l * 16;
#define F_GATHER fg[0] = ld<AUX_SC1>(rS, a_f, 0); fg[1] = ld<AUX_SC1>(rS, a_f + 1024, 0);
#define F_OK __all((fg[0].x & fg[0].z & fg[1].x & fg[1].z & 1u) != 0u)
      F_GATHER
      RETRY(F_OK, 20, F_GATHER)
      f32x4 a = __builtin_bit_cast(f32x4, fg[0]) + __builtin_bit_cast(f32x4, fg[1]);
#pragma unroll
      for (int o = 8; o < 64; o <<= 1) {
#pragma unroll
        for (int e = 0; e < 4; ++e) a[e] += __shfl_xor(a[e], o);
      }
      if (l < 8) *(LAS f32x4*)(L + L_FIN + (w * 8 + l) * 16) = a;
      arrive(L, C_FIN, l);
      if (w == 0) {
        await<0>(L, C_FIN, 8);
        if (l < 32) {
          float t = 0.f;
#pragma unroll
          for (int ww = 0; ww < 8; ++ww) t += ((LAS float*)(L + L_FIN))[ww * 32 + l];
          __builtin_amdgcn_raw_buffer_store_b16((unsigned short)pack_bf16(t, 0.f), rO, ((l & 1) * 896 + br * 448 + 16 * mblk + (l >> 1)) * 2, 0, 0);
          if (mblk == 0 && l == 0) __builtin_amdgcn_raw_buffer_store_b32(tag, rS, EP_OFF + 256 * br, 0, AUX_SC1);
        }
      }
      STAMP(9);
    }
  } else {
    const int w = wv;
    __builtin_amdgcn_s_setprio(2);
    const rsrc_t rUK = __builtin_amdgcn_make_buffer_rsrc((void*)p.in[br * 6 + 3], 0, 1024 * 4096 * 2, 0x00020000);
    const rsrc_t rUV = __builtin_amdgcn_make_buffer_rsrc((void*)p.in[br * 6 + 4], 0, 1024 * 4096 * 2, 0x00020000);
    const rsrc_t rWO = __builtin_amdgcn_make_buffer_rsrc((void*)p.in[br * 6 + 5], 0, 4096 * 448 * 2, 0x00020000);
    v4u uk[2][8];
    v4u qg[2];
    const int a_qg = Q_OFF + (br * 16 + h) * 8192 + w * 1024 + l * 16;
#define Q_GATHER qg[0] = ld<AUX_SC1>(rS, a_qg, 0); qg[1] = ld<AUX_SC1>(rS, a_qg, 4096);
    {
      LAS unsigned char* st = L + L_RED + w * 16384;
      const int vo = (((128 * j + 32 * w + (l >> 5)) * 16 + h) * 256) * 2 + (l & 31) * 16;
      v4u raw[2][8];
      asm volatile("s_waitcnt lgkmcnt(0)" ::: "memory");
      __builtin_amdgcn_s_barrier();
      asm volatile("" ::: "memory");
#pragma unroll
      for (int ct = 0; ct < 2; ++ct)
#pragma unroll
        for (int n = 0; n < 8; ++n) raw[ct][n] = ld<AUX_W>(rUK, vo, (16 * ct + 2 * n) * 8192);
      PIN();
      STAMP(10);
      {
        const v4u z = {0u, 0u, 0u, 0u};
        st_sc1(z, rS, O_OFF + ((br * 16 + 4 * w + g) * 16 + col_own) * 2048 + i * 256 + r * 16);
        st_sc1(z, rS, w < 2 ? P_OFF + (prod * 2 + w) * 1024 + l * 16 : ((w == 2 && l < 16) ? PS_OFF + prod * 256 + l * 16 : OOB));
        st_sc1(v2u{0u, 0u}, rS, (w == 3 && l < 16) ? OS_OFF + (((br * 16 + r) * 8 + i) * 16 + col_own) * 8 : OOB);
        const int mb = 8 * w + (l >> 3);
        st_sc1(z, rS, mb < 28 ? OUT_OFF + ((br * 28 + mb) * 128 + h * 8 + j) * 128 + (l & 7) * 16 : OOB);
      }
      PIN();
      Q_GATHER
      PIN();
      if (l == 0) __hip_atomic_fetch_add((LAS unsigned*)(L + L_CNT) + C_FRONT, 1u, __ATOMIC_RELAXED, __HIP_MEMORY_SCOPE_WORKGROUP);
#pragma unroll
      for (int ct = 0; ct < 2; ++ct) {
#pragma unroll
        for (int n = 0; n < 8; ++n) {
          const int row = 2 * n + (l >> 5);
          *(LAS v4u*)(st + ct * 8192 + row * 512 + (((l & 31) ^ row) * 16)) = raw[ct][n];
        }
#pragma unroll
        for (int n = 0; n < 8; ++n) uk[ct][n] = *(LAS v4u*)(st + ct * 8192 + r * 512 + (((4 * n + g) ^ r) * 16));
        if (ct == 0) {
          asm volatile("" : "+v"(uk[0][0]), "+v"(uk[0][1]), "+v"(uk[0][2]), "+v"(uk[0][3]), "+v"(uk[0][4]), "+v"(uk[0][5]), "+v"(uk[0][6]), "+v"(uk[0][7]) :: "memory");
          PIN();
        }
      }
    }
    unsigned tag = (unsigned)__builtin_amdgcn_readfirstlane(ep) + 1u;
    if (tag == 0u) tag = 1u;
    __builtin_amdgcn_s_setprio(3);
    {
      RETRY(tags_ok<2>(qg, tag), 16, Q_GATHER)
      STAMP(11);
      *(LAS unsigned*)(L + L_Q + w * 256 + l * 4) = pack_bf16(uf(qg[0].x) + uf(qg[1].x), uf(qg[0].z) + uf(qg[1].z));
      arrive(L, C_Q, l);
      await<0>(L, C_Q, 4);
      f32x4 acc[2] = {{0.f, 0.f, 0.f, 0.f}, {0.f, 0.f, 0.f, 0.f}};
#pragma unroll
      for (int n = 0; n < 8; ++n) {
        const v4u qf = *(LAS v4u*)(L + L_Q + (l & 1) * 512 + g * 16 + n * 64);
        acc[0] = mfma(uk[0][n], qf, acc[0]);
        acc[1] = mfma(uk[1][n], qf, acc[1]);
      }
      const float SC = 1.4426950408889634f * 0.0625f;
#pragma unroll
      for (int ct = 0; ct < 2; ++ct) {
        const v2u u = {pack_bf16(acc[ct][0] * SC, acc[ct][1] * SC), pack_bf16(acc[ct][2] * SC, acc[ct][3] * SC)};
        st_sc1(u, rS, r < 2 ? QA_OFF + ((((br * 2 + r) * 8 + j) * 4 + w) * 64 + h + 16 * (2 * ct + (g >> 1))) * 16 + (g & 1) * 8 : OOB);
      }
      asm volatile("s_waitcnt vmcnt(0)" ::: "memory");
      __builtin_amdgcn_raw_buffer_store_b32(tag, rS, l == 0 ? QF_OFF + (((br * 8 + j) * 16 + h) * 4 + w) * 4 : OOB, 0, AUX_SC1);
    }
    STAMP(12);
    {
      v4u fl;
      const int a_fl = QF_OFF + (br * 8 + 2 * w) * 256 + (l & 31) * 16;
#define FL_GATHER fl = ld<AUX_SC1>(rS, a_fl, 0);
#define FL_OK __all(((fl.x ^ tag) | (fl.y ^ tag) | (fl.z ^ tag) | (fl.w ^ tag)) == 0u)
      FL_GATHER
      RETRY(FL_OK, 17, FL_GATHER)
      asm volatile("" ::: "memory");
      __builtin_amdgcn_s_setprio(2);
      v4u qa[8];
      const int a_qa = QA_OFF + ((br * 2 + b) * 8 + 2 * w) * 4096 + l * 16;
#pragma unroll
      for (int n = 0; n < 8; ++n) qa[n] = ld<AUX_SC1>(rS, a_qa, n * 1024);
#pragma unroll
      for (int n = 0; n < 8; ++n) *(LAS v4u*)(L + L_QA + ((8 * w + n) * 64 + l) * 16) = qa[n];
    }
    arrive(L, C_QA, l);
    STAMP(13);
#define ISS_GATE 8
    await<2>(L, C_ISS, ISS_GATE);
    __builtin_amdgcn_s_setprio(0);
    v4u uv[4][4];
    {
      LAS unsigned char* st = L + L_ST1 + w * 4096;
      v4u raw[4][4], wo[14];
      const int vo_uv = (((512 * hf + 128 * w + (l >> 3)) * 16 + h) * 256 + 64 * dq) * 2 + (l & 7) * 16;
      const int vo_wo = (h * 256 + 64 * dq) * 896 + w * 14336 + l * 16;
#pragma unroll
      for (int ks = 0; ks < 4; ++ks)
#pragma unroll
        for (int n = 0; n < 4; ++n) raw[ks][n] = ld<AUX_W>(rUV, vo_uv, (32 * ks + 8 * n) * 8192);
      PIN();
#pragma unroll
      for (int n = 0; n < 7; ++n) wo[n] = ld<0>(rWO, vo_wo, n * 1024);
      PIN();
#define RELAY_UV(ks) \
      _Pragma("unroll") for (int n = 0; n < 4; ++n) { \
        const int row = 8 * n + (l >> 3), ch = l & 7, f = ((row >> 1) & 1) | (((row >> 3) & 1) << 1); \
        *(LAS v4u*)(st + row * 128 + (((ch >> 1) ^ f) * 32) + (ch & 1) * 16) = raw[ks][n]; \
      } \
      _Pragma("unroll") for (int dt = 0; dt < 4; ++dt) uv[ks][dt] = tr_frag(st + t128_r + ((dt ^ t128_f) * 32), 512); \
      asm volatile("" : "+v"(uv[ks][0]), "+v"(uv[ks][1]), "+v"(uv[ks][2]), "+v"(uv[ks][3]) :: "memory"); \
      PIN();
      RELAY_UV(0)
      RELAY_UV(1)
#pragma unroll
      for (int n = 7; n < 14; ++n) wo[n] = ld<0>(rWO, vo_wo, n * 1024);
      PIN();
      STAMP(14);
      __builtin_amdgcn_s_setprio(3);
      RELAY_UV(2)
      RELAY_UV(3)
      await<1>(L, C_SDONE, 2);
#pragma unroll
      for (int n = 0; n < 14; ++n) {
        const int o = w * 14336 + n * 1024 + l * 16, row = o / 896, ch = (o - row * 896) >> 4;
        const int f = ((row >> 1) & 1) | (((row >> 3) & 1) << 1);
        *(LAS v4u*)(L + L_WO + row * 896 + (((ch >> 1) ^ f) * 32) + (ch & 1) * 16) = wo[n];
      }
      arrive(L, C_WO, l);
    }
    STAMP(15);
    {
      v4u og[4];
      v2u os;
      const int a_og = O_OFF + ((br * 16 + h) * 16 + r) * 2048 + (4 * hf + w) * 256 + g * 16;
      const int a_osg = OS_OFF + (((br * 16 + h) * 8 + 4 * hf + w) * 16 + r) * 8;
#define O_GATHER \
      _Pragma("unroll") for (int n = 0; n < 4; ++n) og[n] = ld<AUX_SC1>(rS, a_og + n * 64, 0); \
      os = __builtin_amdgcn_raw_buffer_load_b64(rS, a_osg, 0, AUX_SC1);
#define O_OK __all((og[0].x & og[0].z & og[1].x & og[1].z & og[2].x & og[2].z & og[3].x & og[3].z & os.y & 1u) != 0u)
      O_GATHER
      RETRY(O_OK, 19, O_GATHER)
      STAMP(21);
      const float Mo = uf(os.x), Lo = uf(os.y);
      const float Mb = max8(Mo);
      const float f = __builtin_amdgcn_exp2f(Mo - Mb);
      const float Lb = sum8(f * Lo);
      const float fac = f / Lb;
      f32x4 acc[4];
#pragma unroll
      for (int dt = 0; dt < 4; ++dt) acc[dt] = f32x4{0.f, 0.f, 0.f, 0.f};
#pragma unroll
      for (int ks = 0; ks < 4; ++ks) {
#pragma unroll
        for (int dt = 0; dt < 4; ++dt) acc[dt] = mfma(uv[ks][dt], og[ks], acc[dt]);
      }
#pragma unroll
      for (int dt = 0; dt < 4; ++dt) {
        f32x4 v;
#pragma unroll
        for (int rr = 0; rr < 4; ++rr) v[rr] = sum8(acc[dt][rr] * fac);
        if ((l & 7) == 0) *(LAS f32x4*)(L + L_CX + ((w * 2 + (r >> 3)) * 64 + 16 * dt + 4 * g) * 4) = v;
      }
    }
    arrive(L, C_CX, l);
    {
      await<0>(L, C_WO, 4);
      v4u wof[7][2];
#pragma unroll
      for (int mt = 0; mt < 7; ++mt)
#pragma unroll
        for (int ks = 0; ks < 2; ++ks)
          wof[mt][ks] = tr_frag(L + L_WO + (32 * ks + 8 * g + q4) * 896 + (((7 * w + mt) ^ t128_f) * 32) + p4 * 8, 4 * 896);
      await<0>(L, C_CX, 4);
      v4u af[2];
#pragma unroll
      for (int ks = 0; ks < 2; ++ks) {
        f32x4 c0 = {0.f, 0.f, 0.f, 0.f}, c1 = {0.f, 0.f, 0.f, 0.f};
#pragma unroll
        for (int ww = 0; ww < 4; ++ww) {
          c0 += *(LAS f32x4*)(L + L_CX + ((ww * 2 + (l & 1)) * 64 + 32 * ks + 8 * g) * 4);
          c1 += *(LAS f32x4*)(L + L_CX + ((ww * 2 + (l & 1)) * 64 + 32 * ks + 8 * g + 4) * 4);
        }
        v4u a = {pack_bf16(c0[0], c0[1]), pack_bf16(c0[2], c0[3]), pack_bf16(c1[0], c1[1]), pack_bf16(c1[2], c1[3])};
        if (l & 2) {
          a = v4u{pack_bf16(c0[0] - bf_lo(a.x), c0[1] - bf_hi(a.x)), pack_bf16(c0[2] - bf_lo(a.y), c0[3] - bf_hi(a.y)),
                  pack_bf16(c1[0] - bf_lo(a.z), c1[1] - bf_hi(a.z)), pack_bf16(c1[2] - bf_lo(a.w), c1[3] - bf_hi(a.w))};
        }
        af[ks] = a;
      }
#pragma unroll
      for (int mt = 0; mt < 7; ++mt) {
        f32x4 d = mfma(af[0], wof[mt][0], f32x4{0.f, 0.f, 0.f, 0.f});
        d = mfma(af[1], wof[mt][1], d);
        st_sc1(v2u{fu(d[0] + d[2]) | 1u, fu(d[1] + d[3])}, rS, l < 16 ? OUT_OFF + ((br * 28 + 7 * w + mt) * 128 + h * 8 + j) * 128 + l * 8 : OOB);
      }
    }
    arrive(L, C_OUT, l);
    STAMP(22);
  }
}
}

extern "C" void kernel_launch(void* const* d_in, const int* in_sizes, int n_in, void* d_out, int out_size, void* d_ws, size_t ws_size, hipStream_t stream) {
  mla::Params p;
  for (int k = 0; k < 12; ++k) p.in[k] = d_in[k];
  p.ws = d_ws;
  p.out = d_out;
  (void)hipFuncSetAttribute((const void*)mla::mla_kernel, hipFuncAttributeMaxDynamicSharedMemorySize, mla::LDS_BYTES);
  hipLaunchKernelGGL(mla::mla_kernel, dim3(mla::NWG), dim3(mla::NTHR), mla::LDS_BYTES, stream, p);
}
```

```cpp
#include <hip/hip_runtime.h>
#include <hip/hip_bf16.h>
#include <stdint.h>

namespace mla {
typedef unsigned v4u __attribute__((ext_vector_type(4)));
typedef unsigned v2u __attribute__((ext_vector_type(2)));
typedef float f32x4 __attribute__((ext_vector_type(4)));
typedef float f32x2 __attribute__((ext_vector_type(2)));
typedef short bf16x8 __attribute__((ext_vector_type(8)));
typedef short s16x4 __attribute__((ext_vector_type(4)));
typedef __bf16 bf16x2_t __attribute__((ext_vector_type(2)));
#define LAS __attribute__((address_space(3)))
#define GAS __attribute__((address_space(1)))
typedef __amdgpu_buffer_rsrc_t rsrc_t;

constexpr int NWG = 256, NTHR = 768;
constexpr int AUX_SC1 = 16;
#define AUX_W 0
#define AUX_C 0
constexpr int EP_OFF = 0;
constexpr int QF_OFF = 1024;
constexpr int Q_OFF = 8192;
constexpr int QA_OFF = Q_OFF + 262144;
constexpr int P_OFF = QA_OFF + 262144;
constexpr int PS_OFF = P_OFF + 524288;
constexpr int O_OFF = PS_OFF + 131072;
constexpr int OS_OFF = O_OFF + 1048576;
constexpr int OUT_OFF = OS_OFF + 32768;
constexpr int WS_BYTES = OUT_OFF + 2 * 28 * 16384;
constexpr int L_PARK = 0;
constexpr int L_ST2 = 32768;
constexpr int L_WO = 0;
constexpr int L_RED = 65536;
constexpr int L_ST1 = 131072;
constexpr int L_P1 = 147456;
constexpr int L_CX = L_P1 + 4096;
constexpr int L_FIN = L_CX + 2304;
constexpr int L_ML = L_FIN + 1024;
constexpr int L_Q = L_ML + 1024;
constexpr int L_MX = L_Q + 1024;
constexpr int L_CNT = L_MX + 256;
constexpr int LDS_BYTES = L_CNT + 128;
enum { C_P1 = 0, C_S, C_ISS, C_PV, C_WO, C_CX, C_FIN, C_Q, C_FRONT, C_MX, C_PG, C_PP, C_OP, C_N };
constexpr unsigned SPIN_CAP = 1u << 14;
constexpr unsigned LDS_SPIN_CAP = 1u << 20;
constexpr int OOB = 0x3FF00000;

struct Params { const void* in[12]; void* ws; void* out; };

__device__ __forceinline__ f32x4 mfma(v4u a, v4u b, f32x4 c) {
  return __builtin_amdgcn_mfma_f32_16x16x32_bf16(__builtin_bit_cast(bf16x8, a), __builtin_bit_cast(bf16x8, b), c, 0, 0, 0);
}
__device__ __forceinline__ unsigned pack_bf16(float a, float b) {
  f32x2 v = {a, b};
  return __builtin_bit_cast(unsigned, __builtin_convertvector(v, bf16x2_t));
}
__device__ __forceinline__ float bf_lo(unsigned u) { return __builtin_bit_cast(float, u << 16); }
__device__ __forceinline__ float bf_hi(unsigned u) { return __builtin_bit_cast(float, u & 0xffff0000u); }
__device__ __forceinline__ unsigned fu(float f) { return __builtin_bit_cast(unsigned, f); }
__device__ __forceinline__ float uf(unsigned u) { return __builtin_bit_cast(float, u); }
template <int AUX> __device__ __forceinline__ v4u ld(rsrc_t rs, int voff, int soff) {
  return __builtin_amdgcn_raw_buffer_load_b128(rs, voff, soff, AUX);
}
__device__ __forceinline__ void st_sc1(v4u v, rsrc_t rs, int voff) { __builtin_amdgcn_raw_buffer_store_b128(v, rs, voff, 0, AUX_SC1); }
__device__ __forceinline__ void st_sc1(v2u v, rsrc_t rs, int voff, int soff = 0) { __builtin_amdgcn_raw_buffer_store_b64(v, rs, voff, soff, AUX_SC1); }
__device__ __forceinline__ v2u tr_read(LAS unsigned char* p) {
  return __builtin_bit_cast(v2u, __builtin_amdgcn_ds_read_tr16_b64_v4i16((LAS s16x4*)p));
}
__device__ __forceinline__ v4u tr_frag(LAS unsigned char* p, int hi_off) {
  const v2u lo = tr_read(p), hi = tr_read(p + hi_off);
  return v4u{lo.x, lo.y, hi.x, hi.y};
}
template <int CTRL> __device__ __forceinline__ float dpp(float v) {
  return __builtin_bit_cast(float, __builtin_amdgcn_update_dpp(0, __builtin_bit_cast(int, v), CTRL, 0xf, 0xf, true));
}
__device__ __forceinline__ float sum_x16(float v) { const auto r = __builtin_amdgcn_permlane16_swap(fu(v), fu(v), false, false); return uf(r[0]) + uf(r[1]); }
__device__ __forceinline__ float sum_x32(float v) { const auto r = __builtin_amdgcn_permlane32_swap(fu(v), fu(v), false, false); return uf(r[0]) + uf(r[1]); }
__device__ __forceinline__ float max_x16(float v) { const auto r = __builtin_amdgcn_permlane16_swap(fu(v), fu(v), false, false); return fmaxf(uf(r[0]), uf(r[1])); }
__device__ __forceinline__ float max_x32(float v) { const auto r = __builtin_amdgcn_permlane32_swap(fu(v), fu(v), false, false); return fmaxf(uf(r[0]), uf(r[1])); }
#define PIN() __builtin_amdgcn_sched_barrier(0)
#define KEEP(x) asm volatile("" : "+v"(x))
__device__ __forceinline__ void arrive(LAS unsigned char* L, int c) {
  asm volatile("s_mov_b64 exec, 1\n\tds_add_u32 %0, %1\n\ts_mov_b64 exec, -1" :: "v"((unsigned)(size_t)(L + L_CNT + 4 * c)), "v"(1u) : "memory");
}
template <int SLEEP> __device__ __forceinline__ void await(LAS unsigned char* L, int c, unsigned n) {
  asm volatile("" ::: "memory");
#pragma nounroll
  for (unsigned spins = 0; spins < LDS_SPIN_CAP; ++spins) {
    const unsigned v = __builtin_amdgcn_readfirstlane(__hip_atomic_load((LAS unsigned*)(L + L_CNT) + c, __ATOMIC_RELAXED, __HIP_MEMORY_SCOPE_WORKGROUP));
    if (v >= n) break;
    if (SLEEP) __builtin_amdgcn_s_sleep(SLEEP);
  }
  asm volatile("" ::: "memory");
}
#define RETRY(OK, GATHER) \
  if (!(OK)) { \
    unsigned spins = 0; \
    do { __builtin_amdgcn_s_sleep(POLL_SLEEP); GATHER } while (!(OK) && ++spins < SPIN_CAP); \
  }
#define AUX_O 0
#define SLEEP_F 12
#define WIN_THIN 2
#define WIN1 6
#define WIN1T 8
#define POLL_SLEEP 1

__global__ void __launch_bounds__(NTHR, 3) mla_kernel(Params p) {
  extern __shared__ __attribute__((aligned(16))) unsigned char smem[];
  LAS unsigned char* L = (LAS unsigned char*)smem;
  const int br = (blockIdx.x >> 2) & 1;
  const void* pX = p.in[br * 6 + 0]; const void* pC = p.in[br * 6 + 1]; const void* pWQ = p.in[br * 6 + 2];
  const void* pUK = p.in[br * 6 + 3]; const void* pUV = p.in[br * 6 + 4]; const void* pWO = p.in[br * 6 + 5];
  void* pS = p.ws; void* pO = p.out;
  asm volatile("" : "+s"(pX), "+s"(pC), "+s"(pWQ), "+s"(pUK), "+s"(pUV), "+s"(pWO), "+s"(pS), "+s"(pO));
  const int tid = threadIdx.x, l = tid & 63, wv = __builtin_amdgcn_readfirstlane(tid >> 6);
  const int r = l & 15, g = l >> 4, q4 = r >> 2, p4 = l & 3;
  const int bid = blockIdx.x, x = bid & 7, s = bid >> 3;
  const int h = 4 * (x & 3) + (s >> 3), j = s & 7, dq = j & 3, hf = j >> 2;
  const int b = (x >> 1) & 1, kg = 4 * (x & 1) + (s >> 3), i = s & 7;
  const int grp = (br * 2 + b) * 8 + kg, prod = grp * 8 + i, col_own = b * 8 + kg;
  if (tid < C_N) ((LAS unsigned*)(L + L_CNT))[tid] = 0u;
  const rsrc_t rS = __builtin_amdgcn_make_buffer_rsrc(pS, 0, WS_BYTES, 0x00020000);
  const int t128_w = (l >> 3) * 128 + ((((l & 7) >> 1) ^ ((l >> 4) & 1)) * 32) + (l & 1) * 16;
  const int t128_f = ((q4 >> 1) & 1) | ((g & 1) << 1);

  if (__builtin_expect(wv >= 4, 1)) {
    const int w = wv - 4;
    __builtin_amdgcn_s_setprio(3);
    const rsrc_t rX = __builtin_amdgcn_make_buffer_rsrc((void*)pX, 0, 2 * 448 * 2, 0x00020000);
    const rsrc_t rC = __builtin_amdgcn_make_buffer_rsrc((void*)pC, 0, 2 * 4096 * 1024 * 2, 0x00020000);
    const rsrc_t rWQ = __builtin_amdgcn_make_buffer_rsrc((void*)pWQ, 0, 448 * 4096 * 2, 0x00020000);
    const rsrc_t rO = __builtin_amdgcn_make_buffer_rsrc(pO, 0, 1792 * 2, 0x00020000);
    LAS unsigned char* st = L + L_ST2 + w * 4096;
    const int vo1 = ((b * 4096 + 512 * kg + 64 * i + 4 * g) * 1024 + 128 * w + 8 * r) * 2;
    const int vo2 = ((b * 4096 + 512 * kg + 64 * w + 4 * g) * 1024 + 128 * i + 8 * r) * 2;
    unsigned ep;
    {
      const int m0 = 224 * hf + 32 * w;
      const int vo = w < 7 ? (m0 + (l >> 3)) * 8192 + (256 * h + 64 * dq) * 2 + (l & 7) * 16 : OOB;
      v4u wq[4];
#pragma unroll
      for (int n = 0; n < 4; ++n) wq[n] = ld<AUX_W>(rWQ, vo, n * 8 * 8192);
      PIN();
      const v4u xf = ld<0>(rX, w < 7 ? ((l & 1) * 448 + m0 + 8 * g) * 2 : OOB, 0);
      ep = __builtin_amdgcn_raw_buffer_load_b32(rS, EP_OFF + 256 * br, 0, AUX_SC1);
      PIN();
      asm volatile("s_waitcnt lgkmcnt(0)" ::: "memory");
      __builtin_amdgcn_s_barrier();
      asm volatile("" ::: "memory");
#pragma unroll
      for (int n = 0; n < 4; ++n) *(LAS v4u*)(st + (t128_w ^ ((n & 1) * 64)) + n * 1024) = wq[n];
      LAS float* o = (LAS float*)(L + L_P1) + w * 128 + l;
      f32x4 a[4];
#pragma unroll
      for (int dt = 0; dt < 4; ++dt) a[dt] = mfma(xf, tr_frag(st + (8 * g + q4) * 128 + p4 * 8 + ((dt ^ t128_f) * 32), 512), f32x4{0.f, 0.f, 0.f, 0.f});
      if (l < 16) {
#pragma unroll
        for (int dt = 0; dt < 4; ++dt) { o[16 * dt] = a[dt][0]; o[64 + 16 * dt] = a[dt][1]; }
      }
      arrive(L, C_P1);
    }
    unsigned tag = (unsigned)__builtin_amdgcn_readfirstlane(ep) + 1u;
    if (tag == 0u) tag = 1u;
    if (w == 7) {
      f32x2 a = {0.f, 0.f};
      await<0>(L, C_P1, 8);
#pragma unroll
      for (int ww = 0; ww < 7; ++ww) a += *(LAS f32x2*)(L + L_P1 + (ww * 128 + 2 * l) * 4);
      st_sc1(v4u{fu(a.x), tag, fu(a.y), tag}, rS, Q_OFF + ((((br * 16 + h) * 2 + hf) * 2 + (l >> 5)) * 128 + 32 * dq + (l & 31)) * 16);
    }
    PIN();
    __builtin_amdgcn_s_setprio(1);
    v4u raw[2][4], v1[4][4], v2[2][8], vt[2][8];
#define WIN2 5
#define WIN(n) asm volatile("s_waitcnt vmcnt(%0)" :: "i"((n) - 1) : "memory")
#define ISSUE_C(kt) \
    _Pragma("unroll") for (int n = 0; n < 4; ++n) { WIN(WIN2); raw[(kt) & 1][n] = ld<AUX_C>(rC, vo1, (16 * (kt) + n) * 2048); PIN(); \
      WIN(WIN2); v2[(kt) >> 1][4 * ((kt) & 1) + n] = ld<AUX_C>(rC, vo2, (16 * (kt) + n) * 2048); PIN(); }
#define RELAY_C(kt) RELAY_CW(kt) RELAY_CR(kt)
#define STB(kt) ((kt) == 2 ? st - L_ST2 + L_PARK : st)
#define RELAY_CW(kt) \
    _Pragma("unroll") for (int n = 0; n < 4; ++n) *(LAS v4u*)(STB(kt) + (4 * g + n) * 256 + ((r ^ (4 * g + n)) * 16)) = raw[(kt) & 1][n]; \
    PIN();
#define RELAY_CR(kt) \
    _Pragma("unroll") for (int ks = 0; ks < 4; ++ks) v1[kt][ks] = *(LAS v4u*)(STB(kt) + r * 256 + (((4 * ks + g) ^ r) * 16)); \
    asm volatile("" : "+v"(v1[kt][0]), "+v"(v1[kt][1]), "+v"(v1[kt][2]), "+v"(v1[kt][3]) :: "memory"); \
    PIN();
#define TRANSPOSE(ks2) \
    _Pragma("unroll") for (int qq = 0; qq < 4; ++qq) \
      _Pragma("unroll") for (int n = 0; n < 4; ++n) { \
        vt[ks2][2 * qq][n] = __builtin_amdgcn_perm(v2[ks2][2 * n + 1][qq], v2[ks2][2 * n][qq], 0x05040100u); \
        vt[ks2][2 * qq + 1][n] = __builtin_amdgcn_perm(v2[ks2][2 * n + 1][qq], v2[ks2][2 * n][qq], 0x07060302u); \
      } \
    asm volatile("" : "+v"(vt[ks2][0]), "+v"(vt[ks2][1]), "+v"(vt[ks2][2]), "+v"(vt[ks2][3]), "+v"(vt[ks2][4]), "+v"(vt[ks2][5]), "+v"(vt[ks2][6]), "+v"(vt[ks2][7])); \
    PIN();
    await<1>(L, C_FRONT, 4);
    ISSUE_C(0)
    ISSUE_C(1)
    RELAY_C(0)
    ISSUE_C(2)
    RELAY_C(1)
    v4u fl;
#define FL_GATHER fl = ld<AUX_SC1>(rS, r * 16, QF_OFF + (br * 8 + w) * 256);
#define FL_OK __all(((fl.x ^ tag) | (fl.y ^ tag) | (fl.z ^ tag) | (fl.w ^ tag)) == 0u)
    FL_GATHER
    PIN();
    ISSUE_C(3)
    RELAY_CW(2)
    v4u qf[4];
    RETRY(FL_OK, FL_GATHER)
    asm volatile("" ::: "memory");
#pragma unroll
    for (int ks = 0; ks < 4; ++ks) qf[ks] = ld<AUX_SC1>(rS, l * 16 + ks * 1024, QA_OFF + ((br * 2 + b) * 8 + w) * 4096);
    PIN();
    arrive(L, C_ISS);
    __builtin_amdgcn_s_setprio(3);
    RELAY_CW(3)
    RELAY_CR(2)
    RELAY_CR(3)
    const int so_p = P_OFF + (prod * 2 + (w >> 1)) * 1024 + (w & 1) * 8;
    const int so_ps = PS_OFF + prod * 512 + w * 8;
    const int so_pg = P_OFF + (grp * 8 + w) * 2048, so_psg = PS_OFF + (grp * 8 + w) * 512;
    const int so_o = O_OFF + ((br * 16 + 2 * w) * 16 + col_own) * 2048 + i * 256;
    const int so_os = OS_OFF + (((br * 16 + 2 * w) * 8 + i) * 16 + col_own) * 8;
#pragma unroll
    for (int kp = 0; kp < 2; ++kp) {
      f32x4 sacc[2];
#pragma unroll
      for (int kt = 0; kt < 2; ++kt) sacc[kt] = mfma(v1[2 * kp + kt][0], qf[0], f32x4{0.f, 0.f, 0.f, 0.f});
#pragma unroll
      for (int ks = 1; ks < 4; ++ks)
#pragma unroll
        for (int kt = 0; kt < 2; ++kt) sacc[kt] = mfma(v1[2 * kp + kt][ks], qf[ks], sacc[kt]);
#pragma unroll
      for (int kt = 0; kt < 2; ++kt) *(LAS f32x4*)(st + ((2 * kp + kt) * 64 + l) * 16) = sacc[kt];
    }
    arrive(L, C_S);
    {
      if (w < 4) {
        await<0>(L, C_S, 8);
        f32x4 sv[8];
#pragma unroll
        for (int ww = 0; ww < 8; ++ww) sv[ww] = *(LAS f32x4*)(L + L_ST2 + ww * 4096 + (w * 64 + l) * 16);
        const f32x4 s0 = ((sv[0] + sv[1]) + (sv[2] + sv[3])) + ((sv[4] + sv[5]) + (sv[6] + sv[7]));
        float m = fmaxf(fmaxf(s0[0], s0[1]), fmaxf(s0[2], s0[3]));
        m = max_x32(max_x16(m));
        if (l < 16) *(LAS float*)(L + L_MX + (l * 4 + w) * 4) = m;
        arrive(L, C_MX);
        await<0>(L, C_MX, 4);
        const f32x4 mx = *(LAS f32x4*)(L + L_MX + r * 16);
        m = fmaxf(fmaxf(mx[0], mx[1]), fmaxf(mx[2], mx[3]));
        float e[4];
#pragma unroll
        for (int k = 0; k < 4; ++k) e[k] = __builtin_amdgcn_exp2f(s0[k] - m);
        st_sc1(v2u{pack_bf16(e[0], e[1]) | 0x8000u, pack_bf16(e[2], e[3])}, rS, l * 16, so_p);
        const float ls = sum_x32(sum_x16((e[0] + e[1]) + (e[2] + e[3])));
        st_sc1(v2u{fu(m), fu(ls) | 1u}, rS, l < 16 ? l * 32 : OOB, so_ps);
        arrive(L, C_PP);
      }
    }
    v4u pg[4];
#define P_GATHER pg[0] = ld<AUX_SC1>(rS, l * 16, so_pg); pg[1] = ld<AUX_SC1>(rS, l * 16 + 1024, so_pg); pg[2] = ld<AUX_SC1>(rS, r * 32, so_psg); pg[3] = ld<AUX_SC1>(rS, r * 32 + 16, so_psg);
#define P_OK __all(((pg[0].x & pg[0].z & pg[1].x & pg[1].z & 0x8000u) != 0u) && ((pg[2].y & pg[2].w & pg[3].y & pg[3].w & 1u) != 0u))
    await<1>(L, C_PP, 4);
#define SLEEP_P 34
    __builtin_amdgcn_s_sleep(SLEEP_P);
    P_GATHER
    PIN();
    TRANSPOSE(0)
    TRANSPOSE(1)
    RETRY(P_OK, P_GATHER)
    arrive(L, C_PG);
    {
      const v4u pf0 = {pg[0].x & ~0x8000u, pg[0].y, pg[0].z & ~0x8000u, pg[0].w}, pf1 = {pg[1].x & ~0x8000u, pg[1].y, pg[1].z & ~0x8000u, pg[1].w};
      f32x4 a0[8];
#pragma unroll
      for (int ii = 0; ii < 8; ++ii) a0[ii] = mfma(vt[0][ii], pf0, f32x4{0.f, 0.f, 0.f, 0.f});
#pragma unroll
      for (int ii = 0; ii < 8; ++ii) a0[ii] = mfma(vt[1][ii], pf1, a0[ii]);
      if (l < 16) *(LAS f32x2*)(L + L_ML + (w * 16 + l) * 8) = f32x2{uf(pg[2].x), (uf(pg[2].y) + uf(pg[2].w)) + (uf(pg[3].y) + uf(pg[3].w))};
#pragma unroll
      for (int ii = 0; ii < 8; ++ii)
        *(LAS f32x4*)(L + L_RED + ((w * 8 + ii) * 64 + 16 * g + ((r + ii + 8 * (g & 1)) & 15)) * 16) = a0[ii];
    }
    arrive(L, C_PV);
    await<0>(L, C_PV, 8);
    {
      const int oh = 2 * w + (l >> 5), oQ = l & 31, slot = oQ & 7, gq = oQ >> 3;
      const int a = L_RED + (slot * 64 + 16 * gq + ((oh + slot + 8 * (gq & 1)) & 15)) * 16;
      f32x2 ml[8];
      f32x4 pv[8];
#pragma unroll
      for (int ww = 0; ww < 8; ++ww) ml[ww] = *(LAS f32x2*)(L + L_ML + (ww * 16 + oh) * 8);
#pragma unroll
      for (int ww = 0; ww < 8; ++ww) pv[ww] = *(LAS f32x4*)(L + a + ww * 8192);
      const float M = fmaxf(fmaxf(fmaxf(ml[0].x, ml[1].x), fmaxf(ml[2].x, ml[3].x)), fmaxf(fmaxf(ml[4].x, ml[5].x), fmaxf(ml[6].x, ml[7].x)));
      f32x4 v = {0.f, 0.f, 0.f, 0.f};
      float Ls = 0.f;
#pragma unroll
      for (int ww = 0; ww < 8; ++ww) {
        const float f = __builtin_amdgcn_exp2f(ml[ww].x - M);
        v += pv[ww] * f;
        Ls += ml[ww].y * f;
      }
      st_sc1(v2u{pack_bf16(v[0], v[1]) | 1u, pack_bf16(v[2], v[3])}, rS, (l >> 5) * 32768 + oQ * 8, so_o);
      st_sc1(v2u{fu(M), fu(Ls) | 1u}, rS, oQ == 0 ? (l >> 5) * 1024 : OOB, so_os);
      arrive(L, C_OP);
    }
    {
      int a_out = w < 7 ? OUT_OFF + ((br * 28 + 4 * w + g) * 128 + h * 8 + j) * 128 + r * 8 : OOB;
      KEEP(a_out);
      await<1>(L, C_WO, 4);
      const int wl = w < 7 ? w : 6;
      v4u wof[4][2];
      int lz = l;
      KEEP(lz);
      const int gz = lz >> 4, qz = (lz & 15) >> 2, fz = ((qz >> 1) & 1) | ((gz & 1) << 1);
#pragma unroll
      for (int mt = 0; mt < 4; ++mt)
#pragma unroll
        for (int ks = 0; ks < 2; ++ks)
          wof[mt][ks] = tr_frag(L + L_WO + (32 * ks + 8 * gz + qz) * 896 + wl * 128 + ((mt ^ fz) * 32) + (lz & 3) * 8, 4 * 896);
      asm volatile("" : "+v"(wof[0][0]), "+v"(wof[0][1]), "+v"(wof[1][0]), "+v"(wof[1][1]), "+v"(wof[2][0]), "+v"(wof[2][1]), "+v"(wof[3][0]), "+v"(wof[3][1]));
      await<0>(L, C_CX, 4);
      v4u af[2];
#pragma unroll
      for (int ks = 0; ks < 2; ++ks) af[ks] = *(LAS v4u*)(L + L_CX + r * 144 + ks * 64 + g * 16);
      asm volatile("" : "+v"(af[0]), "+v"(af[1]));
      f32x4 d[4];
#pragma unroll
      for (int mt = 0; mt < 4; ++mt) d[mt] = mfma(af[0], wof[mt][0], f32x4{0.f, 0.f, 0.f, 0.f});
#pragma unroll
      for (int mt = 0; mt < 4; ++mt) d[mt] = mfma(af[1], wof[mt][1], d[mt]);
      asm volatile("" : "+v"(d[0]), "+v"(d[1]), "+v"(d[2]), "+v"(d[3]));
      {
        float e0[4], e1[4];
#pragma unroll
        for (int mt = 0; mt < 4; ++mt) { e0[mt] = d[mt][0] + d[mt][2]; e1[mt] = d[mt][1] + d[mt][3]; }
        float f0[2], f1[2];
#pragma unroll
        for (int k = 0; k < 2; ++k) {
          const auto a0 = __builtin_amdgcn_permlane32_swap(fu(e0[k]), fu(e0[2 + k]), false, false);
          const auto a1 = __builtin_amdgcn_permlane32_swap(fu(e1[k]), fu(e1[2 + k]), false, false);
          f0[k] = uf(a0[0]) + uf(a0[1]); f1[k] = uf(a1[0]) + uf(a1[1]);
        }
        const auto b0 = __builtin_amdgcn_permlane16_swap(fu(f0[0]), fu(f0[1]), false, false);
        const auto b1 = __builtin_amdgcn_permlane16_swap(fu(f1[0]), fu(f1[1]), false, false);
        st_sc1(v2u{fu(uf(b0[0]) + uf(b0[1])) | 1u, fu(uf(b1[0]) + uf(b1[1]))}, rS, a_out);
      }
    }
    const int mblk = s * 4 + (x & 3);
    if (mblk < 28) {
      v4u fg[2];
      const int a_f = OUT_OFF + ((br * 28 + mblk) * 128 + 16 * w) * 128 + l * 16;
#define F_GATHER fg[0] = ld<AUX_SC1>(rS, a_f, 0); fg[1] = ld<AUX_SC1>(rS, a_f + 1024, 0);
#define F_OK __all((fg[0].x & fg[0].z & fg[1].x & fg[1].z & 1u) != 0u)
      __builtin_amdgcn_s_sleep(SLEEP_F);
      F_GATHER
      RETRY(F_OK, F_GATHER)
      f32x4 a = __builtin_bit_cast(f32x4, fg[0]) + __builtin_bit_cast(f32x4, fg[1]);
#pragma unroll
      for (int e = 0; e < 4; ++e) a[e] = sum_x32(sum_x16(a[e] + dpp<0x128>(a[e])));
      if (l < 8) *(LAS f32x4*)(L + L_FIN + (w * 8 + l) * 16) = a;
      arrive(L, C_FIN);
      if (w == 0) {
        await<0>(L, C_FIN, 8);
        if (l < 32) {
          float t = 0.f;
#pragma unroll
          for (int ww = 0; ww < 8; ++ww) t += ((LAS float*)(L + L_FIN))[ww * 32 + l];
          __builtin_amdgcn_raw_buffer_store_b16((unsigned short)pack_bf16(t, 0.f), rO, ((l & 1) * 896 + br * 448 + 16 * mblk + (l >> 1)) * 2, 0, 0);
          if (mblk == 0 && l == 0) __builtin_amdgcn_raw_buffer_store_b32(tag, rS, EP_OFF + 256 * br, 0, AUX_SC1);
        }
      }
    }
  } else {
    const int w = wv;
    __builtin_amdgcn_s_setprio(2);
    const rsrc_t rUK = __builtin_amdgcn_make_buffer_rsrc((void*)pUK, 0, 1024 * 4096 * 2, 0x00020000);
    const rsrc_t rUV = __builtin_amdgcn_make_buffer_rsrc((void*)pUV, 0, 1024 * 4096 * 2, 0x00020000);
    const rsrc_t rWO = __builtin_amdgcn_make_buffer_rsrc((void*)pWO, 0, 4096 * 448 * 2, 0x00020000);
    v4u uk[2][8];
    v4u qg[2];
    unsigned ep;
    const int a_qg = Q_OFF + (br * 16 + h) * 8192 + w * 1024 + l * 16;
#define Q_GATHER qg[0] = ld<AUX_SC1>(rS, a_qg, 0); qg[1] = ld<AUX_SC1>(rS, a_qg, 4096);
#define Q_OK __all(((qg[0].y ^ tag) | (qg[0].w ^ tag) | (qg[1].y ^ tag) | (qg[1].w ^ tag)) == 0u)
    {
      LAS unsigned char* st = L + L_RED + w * 16384;
      const int vo = (((128 * j + 32 * w + (l >> 5)) * 16 + h) * 256) * 2 + (l & 31) * 16;
      v4u raw[2][8];
      ep = __builtin_amdgcn_raw_buffer_load_b32(rS, EP_OFF + 256 * br, 0, AUX_SC1);
      PIN();
      asm volatile("s_waitcnt lgkmcnt(0)" ::: "memory");
      __builtin_amdgcn_s_barrier();
      asm volatile("" ::: "memory");
#pragma unroll
      for (int ct = 0; ct < 2; ++ct)
#pragma unroll
        for (int n = 0; n < 8; ++n) { WIN(WIN1); raw[ct][n] = ld<AUX_W>(rUK, vo, (16 * ct + 2 * n) * 8192); PIN(); }
      PIN();
      {
        const v4u z = {0u, 0u, 0u, 0u};
        st_sc1(z, rS, O_OFF + ((br * 16 + 4 * w + g) * 16 + col_own) * 2048 + i * 256 + r * 16);
        if (w < 3) st_sc1(z, rS, w < 2 ? P_OFF + (prod * 2 + w) * 1024 + l * 16 : (l < 32 ? PS_OFF + prod * 512 + l * 16 : OOB));
        if (w == 3) st_sc1(v2u{0u, 0u}, rS, l < 16 ? OS_OFF + (((br * 16 + r) * 8 + i) * 16 + col_own) * 8 : OOB);
        const int mb = 8 * w + (l >> 3);
        st_sc1(z, rS, mb < 28 ? OUT_OFF + ((br * 28 + mb) * 128 + h * 8 + j) * 128 + (l & 7) * 16 : OOB);
      }
      PIN();
      Q_GATHER
      PIN();
      arrive(L, C_FRONT);
#pragma unroll
      for (int ct = 0; ct < 2; ++ct) {
#pragma unroll
        for (int n = 0; n < 8; ++n) {
          const int row = 2 * n + (l >> 5);
          *(LAS v4u*)(st + ct * 8192 + row * 512 + (((l & 31) ^ row) * 16)) = raw[ct][n];
        }
#pragma unroll
        for (int n = 0; n < 8; ++n) uk[ct][n] = *(LAS v4u*)(st + ct * 8192 + r * 512 + (((4 * n + g) ^ r) * 16));
        if (ct == 0) {
          asm volatile("" : "+v"(uk[0][0]), "+v"(uk[0][1]), "+v"(uk[0][2]), "+v"(uk[0][3]), "+v"(uk[0][4]), "+v"(uk[0][5]), "+v"(uk[0][6]), "+v"(uk[0][7]) :: "memory");
          PIN();
        }
      }
    }
    unsigned tag = (unsigned)__builtin_amdgcn_readfirstlane(ep) + 1u;
    if (tag == 0u) tag = 1u;
    __builtin_amdgcn_s_setprio(3);
    {
      int a_qa = r < 2 ? QA_OFF + ((((br * 2 + r) * 8 + j) * 4 + w) * 64 + h + 16 * (g >> 1)) * 16 + (g & 1) * 8 : OOB;
      KEEP(a_qa);
      RETRY(Q_OK, Q_GATHER)
      *(LAS unsigned*)(L + L_Q + w * 256 + l * 4) = pack_bf16(uf(qg[0].x) + uf(qg[1].x), uf(qg[0].z) + uf(qg[1].z));
      arrive(L, C_Q);
      await<0>(L, C_Q, 4);
      f32x4 acc[2] = {{0.f, 0.f, 0.f, 0.f}, {0.f, 0.f, 0.f, 0.f}};
      v4u qf[8];
#pragma unroll
      for (int n = 0; n < 8; ++n) qf[n] = *(LAS v4u*)(L + L_Q + (l & 1) * 512 + g * 16 + n * 64);
#pragma unroll
      for (int n = 0; n < 8; ++n) {
        acc[0] = mfma(uk[0][n], qf[n], acc[0]);
        acc[1] = mfma(uk[1][n], qf[n], acc[1]);
      }
      const float SC = 1.4426950408889634f * 0.0625f;
#pragma unroll
      for (int ct = 0; ct < 2; ++ct) st_sc1(v2u{pack_bf16(acc[ct][0] * SC, acc[ct][1] * SC), pack_bf16(acc[ct][2] * SC, acc[ct][3] * SC)}, rS, a_qa + ct * 512);
      asm volatile("s_waitcnt vmcnt(0)" ::: "memory");
      __builtin_amdgcn_raw_buffer_store_b32(tag, rS, l == 0 ? QF_OFF + (((br * 8 + j) * 16 + h) * 4 + w) * 4 : OOB, 0, AUX_SC1);
    }
#define TAIL_EARLY 12
    await<2>(L, C_ISS, 8);
    __builtin_amdgcn_s_setprio(0);
    v4u uv[4][4];
    {
      v4u og[4], os[2];
      const int a_og = O_OFF + ((br * 16 + h) * 16 + r) * 2048 + (4 * hf + w) * 256 + g * 16;
      const int a_osg = OS_OFF + (((br * 16 + h) * 8 + 4 * hf + w) * 16 + 4 * g) * 8;
#define O_GATHER \
      _Pragma("unroll") for (int n = 0; n < 4; ++n) og[n] = ld<AUX_SC1>(rS, a_og + n * 64, 0); \
      os[0] = ld<AUX_SC1>(rS, a_osg, 0); os[1] = ld<AUX_SC1>(rS, a_osg + 16, 0);
#define O_OK __all((og[0].x & og[0].z & og[1].x & og[1].z & og[2].x & og[2].z & og[3].x & og[3].z & os[0].y & os[0].w & os[1].y & os[1].w & 1u) != 0u)
      LAS unsigned char* st = L + L_ST1 + w * 4096;
      v4u raw[4][4], wo[14];
      const int vo_uv = (((512 * hf + 128 * w + (l >> 3)) * 16 + h) * 256 + 64 * dq) * 2 + (l & 7) * 16;
      const int vo_wo = (h * 256 + 64 * dq) * 896 + w * 14336 + l * 16;
      const int uv_r = (8 * q4 + 2 * g) * 128 + p4 * 8, uv_f = (g & 1) | ((q4 & 1) << 1);
#define RELAY_UV(ks) \
      _Pragma("unroll") for (int n = 0; n < 4; ++n) *(LAS v4u*)(st + (t128_w ^ ((n & 1) * 64)) + n * 1024) = raw[ks][n]; \
      _Pragma("unroll") for (int dt = 0; dt < 4; ++dt) uv[ks][dt] = tr_frag(st + uv_r + ((dt ^ uv_f) * 32), 128); \
      asm volatile("" : "+v"(uv[ks][0]), "+v"(uv[ks][1]), "+v"(uv[ks][2]), "+v"(uv[ks][3]) :: "memory"); \
      PIN();
#define THIN(idx) \
      if ((idx) >= TAIL_EARLY) { \
        if (__builtin_amdgcn_readfirstlane(__hip_atomic_load((LAS unsigned*)(L + L_CNT) + C_PG, __ATOMIC_RELAXED, __HIP_MEMORY_SCOPE_WORKGROUP)) < 8u) { WIN(WIN_THIN); } \
      } \
      WIN(WIN1T);
#pragma unroll
      for (int n = 0; n < 14; ++n) { THIN(n) wo[n] = ld<AUX_O>(rWO, vo_wo, n * 1024); PIN(); }
#pragma unroll
      for (int ks = 0; ks < 2; ++ks)
#pragma unroll
        for (int n = 0; n < 4; ++n) { THIN(14 + 4 * ks + n) raw[ks][n] = ld<AUX_W>(rUV, vo_uv, (32 * ks + 8 * n) * 8192); PIN(); }
      await<1>(L, C_MX, 4);
#pragma unroll
      for (int n = 0; n < 14; ++n) {
        const int o = w * 14336 + n * 1024 + l * 16, row = o / 896, ch = (o - row * 896) >> 4;
        const int f = ((row >> 1) & 1) | (((row >> 3) & 1) << 1);
        *(LAS v4u*)(L + L_WO + row * 896 + (((ch >> 1) ^ f) * 32) + (ch & 1) * 16) = wo[n];
      }
      arrive(L, C_WO);
      PIN();
#pragma unroll
      for (int ks = 2; ks < 4; ++ks)
#pragma unroll
        for (int n = 0; n < 4; ++n) { THIN(14 + 4 * ks + n) raw[ks][n] = ld<AUX_W>(rUV, vo_uv, (32 * ks + 8 * n) * 8192); PIN(); }
      __builtin_amdgcn_s_setprio(3);
      await<1>(L, C_OP, 8);
      O_GATHER
      PIN();
      RELAY_UV(0)
      RELAY_UV(1)
      RELAY_UV(2)
      RELAY_UV(3)
      RETRY(O_OK, O_GATHER)
      f32x4 acc[4];
#pragma unroll
      for (int dt = 0; dt < 4; ++dt) acc[dt] = mfma(og[0], uv[0][dt], f32x4{0.f, 0.f, 0.f, 0.f});
#pragma unroll
      for (int ks = 1; ks < 4; ++ks)
#pragma unroll
        for (int dt = 0; dt < 4; ++dt) acc[dt] = mfma(og[ks], uv[ks][dt], acc[dt]);
      const float M0 = uf(os[0].x), M1 = uf(os[0].z), M2 = uf(os[1].x), M3 = uf(os[1].z);
      const float Mb = max_x16(fmaxf(fmaxf(M0, M1), fmaxf(M2, M3)));
      f32x4 f = {__builtin_amdgcn_exp2f(M0 - Mb), __builtin_amdgcn_exp2f(M1 - Mb), __builtin_amdgcn_exp2f(M2 - Mb), __builtin_amdgcn_exp2f(M3 - Mb)};
      const float Lb = sum_x16((f[0] * uf(os[0].y) + f[1] * uf(os[0].w)) + (f[2] * uf(os[1].y) + f[3] * uf(os[1].w)));
      const float inv = 1.0f / Lb;
      LAS unsigned short* cxp = (LAS unsigned short*)(L + L_CX + (4 * w + 2 * (g & 1) + (g >> 1)) * 144) + r;
#pragma unroll
      for (int dt = 0; dt < 4; ++dt) {
        const float v = sum_x16((acc[dt][0] * f[0] + acc[dt][1] * f[1]) + (acc[dt][2] * f[2] + acc[dt][3] * f[3])) * inv;
        const unsigned hi = pack_bf16(v, 0.f);
        cxp[16 * dt] = (unsigned short)((g & 1) ? pack_bf16(v - bf_lo(hi), 0.f) : hi);
      }
    }
    arrive(L, C_CX);
  }
}
}

extern "C" void kernel_launch(void* const* d_in, const int* in_sizes, int n_in, void* d_out, int out_size, void* d_ws, size_t ws_size, hipStream_t stream) {
  mla::Params p;
  for (int k = 0; k < 12; ++k) p.in[k] = d_in[k];
  p.ws = d_ws;
  p.out = d_out;
  (void)hipFuncSetAttribute((const void*)mla::mla_kernel, hipFuncAttributeMaxDynamicSharedMemorySize, mla::LDS_BYTES);
  hipLaunchKernelGGL(mla::mla_kernel, dim3(mla::NWG), dim3(mla::NTHR), mla::LDS_BYTES, stream, p);
}
```
